```python
import torch

def _softmax(x, axis=-1):
    return torch.softmax(x, dim=axis)
def _gelu(x):
    return torch.nn.functional.gelu(x, approximate="tanh")
def _silu(x):
    return torch.nn.functional.silu(x)
def _sigmoid(x):
    return torch.sigmoid(x)
def _layernorm(x, axis=-1, eps=1e-5):
    m = x.mean(dim=axis, keepdim=True)
    v = x.var(dim=axis, keepdim=True, unbiased=False)
    return (x - m) / torch.sqrt(v + eps)
def _scatter_add(tgt, idx, src, axis=0):
    return tgt.clone().index_add_(axis, idx, src)
def _segment_sum(src, seg, n):
    out = torch.zeros((n,) + src.shape[1:], dtype=src.dtype)
    return out.index_add_(0, seg, src)
def _pad(x, pw):
    flat = tuple(v for pair in reversed(pw) for v in pair)
    return torch.nn.functional.pad(x, flat)

kernel_name = 'standalone__mha_block_cached__near_ridge'

def setup_inputs():

    _w = (lambda a: (lambda t: t.to(torch.bfloat16) if t.is_floating_point() else t)(torch.as_tensor(a)))
    B, Sq, D = 8, 128, 512
    H, Sk, Dh = 32, 128, 128
    return {
        "x": _w((lambda *s: torch.randn(*s, dtype=torch.bfloat16) * 0.8)(B, Sq, D)),
        "k_cache": _w((lambda *s: torch.randn(*s, dtype=torch.bfloat16) * 0.8)(B, H, Sk, Dh)),
        "v_cache": _w((lambda *s: torch.randn(*s, dtype=torch.bfloat16) * 0.05)(B, H, Sk, Dh)),
        "wq": _w((lambda *s: torch.randn(*s, dtype=torch.bfloat16) * 0.8)(D, H * Dh) * (D ** -0.5)),
        "wo": _w((lambda *s: torch.randn(*s, dtype=torch.bfloat16) * 0.05)(H * Dh, D) * ((H * Dh) ** -0.5)),
    }

def reference(x, k_cache, v_cache, wq, wo):
    B, Sq, D = x.shape
    H, Dh = 32, 128
    q = (lambda x, *d: x.permute(*d))((x @ wq).reshape(B, Sq, H, Dh), 0, 2, 1, 3)
    scores = torch.einsum("bhqd,bhkd->bhqk", q, k_cache) * (Dh ** -0.5)
    p = _softmax(scores, axis=-1)
    o = torch.einsum("bhqk,bhkd->bhqd", p, v_cache)
    return (lambda x, *d: x.permute(*d))(o, 0, 2, 1, 3).reshape(B, Sq, H * Dh) @ wo
```

```cpp
#include <hip/hip_runtime.h>
#include <hip/hip_bf16.h>

typedef __attribute__((ext_vector_type(8))) __bf16 bf16x8;
typedef __attribute__((ext_vector_type(4))) __bf16 bf16x4;
typedef __attribute__((ext_vector_type(2))) __bf16 bf16x2;
typedef __attribute__((ext_vector_type(4))) short s16x4;
typedef __attribute__((ext_vector_type(16))) float f32x16;
typedef __attribute__((ext_vector_type(2))) float f32x2;
typedef __attribute__((ext_vector_type(4))) unsigned u32x4;
typedef __attribute__((ext_vector_type(2))) unsigned u32x2;
#define DI __device__ __forceinline__
#define LDSP __attribute__((address_space(3)))
#define MFMA(a, b, c) __builtin_amdgcn_mfma_f32_32x32x16_bf16((a), (b), (c), 0, 0, 0)

DI unsigned cvtpk(float lo, float hi) { f32x2 v = {lo, hi}; bf16x2 b = __builtin_convertvector(v, bf16x2); return __builtin_bit_cast(unsigned, b); }
DI bf16x8 pack8(const f32x16& a, int s) {
  u32x4 w;
  w[0] = cvtpk(a[8 * s + 0], a[8 * s + 1]); w[1] = cvtpk(a[8 * s + 2], a[8 * s + 3]);
  w[2] = cvtpk(a[8 * s + 4], a[8 * s + 5]); w[3] = cvtpk(a[8 * s + 6], a[8 * s + 7]);
  return __builtin_bit_cast(bf16x8, w);
}
DI bf16x8 tr2(const char LDSP* p0, const char LDSP* p1) {
  s16x4 lo = __builtin_amdgcn_ds_read_tr16_b64_v4i16((s16x4 LDSP*)p0);
  s16x4 hi = __builtin_amdgcn_ds_read_tr16_b64_v4i16((s16x4 LDSP*)p1);
  auto v = __builtin_shufflevector(lo, hi, 0, 1, 2, 3, 4, 5, 6, 7);
  return __builtin_bit_cast(bf16x8, v);
}
DI void glds16(const void* g, char LDSP* l) {
  __builtin_amdgcn_global_load_lds((const __attribute__((address_space(1))) void*)g, (LDSP void*)l, 16, 0, 0);
}

__global__ __launch_bounds__(256, 1) void k1_attn(const char* __restrict__ x, const char* __restrict__ kc, const char* __restrict__ vc,
                                                  const char* __restrict__ wq, char* __restrict__ ofr) {
  extern __shared__ __attribute__((aligned(16))) char smem_[];
  char LDSP* lds = (char LDSP*)smem_;
  const int tid = threadIdx.x, lane = tid & 63, w = __builtin_amdgcn_readfirstlane(tid >> 6);
  const int r = lane & 31, hh = lane >> 5, blk = (lane >> 4) & 1, q4 = (lane & 15) >> 2, p = lane & 3;
  const int bid = blockIdx.x, xcd = bid & 7, idx = bid >> 3;
  const int h = xcd * 4 + (idx & 3), b = idx >> 2;
  const char* xb = x + (size_t)b * 131072;
  const char* wqh = wq + (size_t)h * 256;
  const char* kb_ = kc + (size_t)(b * 32 + h) * 32768;
  const char* vb_ = vc + (size_t)(b * 32 + h) * 32768;

  const int grow = (lane >> 2) & 7;
  bf16x8 xf[32];
  #pragma unroll
  for (int ks = 0; ks < 32; ++ks) xf[ks] = *(const bf16x8*)(xb + (size_t)(32 * w + r) * 1024 + ks * 32 + hh * 16);
  #pragma unroll
  for (int i = 0; i < 16; ++i) {
    const int g = 4 * i + w;
    #pragma unroll
    for (int u = 0; u < 2; ++u) {
      const int row = 8 * g + grow;
      const int ch = 4 * (2 * u + (lane >> 5)) + ((lane & 3) ^ ((row >> 2) & 3));
      glds16(wqh + (size_t)row * 8192 + ch * 16, lds + g * 2048 + u * 1024);
    }
  }
  #pragma unroll
  for (int i = 0; i < 4; ++i) {
    const int g = 4 * i + w;
    #pragma unroll
    for (int u = 0; u < 2; ++u) {
      const int row = 8 * g + grow;
      const int ch = 4 * (2 * u + (lane >> 5)) + ((lane & 3) ^ ((row >> 2) & 3));
      glds16(kb_ + row * 256 + ch * 16, lds + 131072 + g * 2048 + u * 1024);
    }
  }
  u32x4 vr[8];
  #pragma unroll
  for (int i = 0; i < 4; ++i) {
    const int g = 4 * i + w;
    #pragma unroll
    for (int u = 0; u < 2; ++u) {
      const int row = 8 * g + grow;
      const int ch = 4 * (2 * u + (lane >> 5)) + ((lane & 3) ^ ((row >> 2) & 3));
      vr[2 * i + u] = *(const u32x4*)(vb_ + row * 256 + ch * 16);
    }
  }
  __syncthreads();
  f32x16 acc[4];
  #pragma unroll
  for (int d = 0; d < 4; ++d) for (int i = 0; i < 16; ++i) acc[d][i] = 0.f;
  {
    const int b0 = 2048 * hh + 64 * q4 + 8 * (p >> 1);
    const int base0 = b0 + 16 * ((2 * blk + (p & 1)) ^ (2 * hh + 0));
    const int base1 = b0 + 256 + 16 * ((2 * blk + (p & 1)) ^ (2 * hh + 1));
    #pragma unroll
    for (int ks = 0; ks < 32; ++ks) {
      #pragma unroll
      for (int db = 0; db < 4; ++db) {
        bf16x8 a = tr2(lds + base0 + 4096 * ks + 512 * db, lds + base1 + 4096 * ks + 512 * db);
        acc[db] = MFMA(a, xf[ks], acc[db]);
      }
    }
  }
  bf16x8 qf[8];
  #pragma unroll
  for (int ks = 0; ks < 8; ++ks) qf[ks] = pack8(acc[ks >> 1], ks & 1);
  __syncthreads();
  #pragma unroll
  for (int i = 0; i < 4; ++i) {
    const int g = 4 * i + w;
    #pragma unroll
    for (int u = 0; u < 2; ++u) *(u32x4 LDSP*)(lds + g * 2048 + u * 1024 + lane * 16) = vr[2 * i + u];
  }
  f32x16 s[4];
  {
    const int kbase0 = 2048 * (r >> 3) + 64 * (r & 7) + 16 * ((0 + hh) ^ ((r >> 2) & 3));
    const int kbase1 = 2048 * (r >> 3) + 64 * (r & 7) + 16 * ((2 + hh) ^ ((r >> 2) & 3));
    #pragma unroll
    for (int kb = 0; kb < 4; ++kb) {
      for (int i = 0; i < 16; ++i) s[kb][i] = 0.f;
      #pragma unroll
      for (int ks = 0; ks < 8; ++ks) {
        bf16x8 a = *(const bf16x8 LDSP*)(lds + 131072 + ((ks & 1) ? kbase1 : kbase0) + 8192 * kb + 512 * (ks >> 1));
        s[kb] = MFMA(a, qf[ks], s[kb]);
      }
    }
  }
  float m = s[0][0];
  #pragma unroll
  for (int kb = 0; kb < 4; ++kb)
    #pragma unroll
    for (int i = 0; i < 16; ++i) m = fmaxf(m, s[kb][i]);
  {
    auto pr = __builtin_amdgcn_permlane32_swap(__float_as_uint(m), __float_as_uint(m), false, false);
    m = fmaxf(__uint_as_float(pr[0]), __uint_as_float(pr[1]));
  }
  const float c = 0.08838834764831845f * 1.4426950408889634f;
  const float mc = m * c;
  float l = 0.f;
  #pragma unroll
  for (int kb = 0; kb < 4; ++kb)
    #pragma unroll
    for (int i = 0; i < 16; ++i) { float e = __builtin_amdgcn_exp2f(s[kb][i] * c - mc); s[kb][i] = e; l += e; }
  {
    auto pr = __builtin_amdgcn_permlane32_swap(__float_as_uint(l), __float_as_uint(l), false, false);
    l = __uint_as_float(pr[0]) + __uint_as_float(pr[1]);
  }
  bf16x8 pf[8];
  #pragma unroll
  for (int ks = 0; ks < 8; ++ks) pf[ks] = pack8(s[ks >> 1], ks & 1);
  __syncthreads();
  f32x16 o[4];
  {
    const int b0 = 256 * hh + 64 * q4 + 8 * (p >> 1);
    const int base0 = b0 + 16 * ((2 * blk + (p & 1)) ^ (0 + hh));
    const int base1 = b0 + 2048 + 16 * ((2 * blk + (p & 1)) ^ (2 + hh));
    #pragma unroll
    for (int db = 0; db < 4; ++db) {
      for (int i = 0; i < 16; ++i) o[db][i] = 0.f;
      #pragma unroll
      for (int ks = 0; ks < 8; ++ks) {
        bf16x8 a = tr2(lds + base0 + 4096 * ks + 512 * db, lds + base1 + 4096 * ks + 512 * db);
        o[db] = MFMA(a, pf[ks], o[db]);
      }
    }
  }
  const float inv = 1.0f / l;
  char* ob = ofr + ((size_t)((b * 4 + w) * 32 + h) * 8) * 1024 + lane * 16;
  #pragma unroll
  for (int ks = 0; ks < 8; ++ks) {
    f32x16 t = o[ks >> 1];
    for (int i = 0; i < 16; ++i) t[i] *= inv;
    *(bf16x8*)(ob + ks * 1024) = pack8(t, ks & 1);
  }
}

__global__ __launch_bounds__(256, 1) void k2_proj(const char* __restrict__ ofr, const char* __restrict__ wo, float* __restrict__ part) {
  extern __shared__ __attribute__((aligned(16))) char smem_[];
  char LDSP* lds = (char LDSP*)smem_;
  const int tid = threadIdx.x, lane = tid & 63, w = __builtin_amdgcn_readfirstlane(tid >> 6);
  const int r = lane & 31, hh = lane >> 5, blk = (lane >> 4) & 1, q4 = (lane & 15) >> 2, p = lane & 3;
  const int bid = blockIdx.x;
  const int kq = bid & 3, nt = (bid >> 2) & 7, b = bid >> 5;
  const char* wos = wo + (size_t)(1024 * kq) * 1024 + nt * 128;
  const int grow = (lane >> 2) & 7;
  #pragma unroll
  for (int i = 0; i < 32; ++i) {
    const int g = 4 * i + w;
    const int row = 8 * g + grow;
    const int ch = 4 * (lane >> 5) + ((lane & 3) ^ ((row >> 2) & 3));
    glds16(wos + (size_t)row * 1024 + ch * 16, lds + g * 1024);
  }
  const char* ob = ofr + ((size_t)((b * 4 + w) * 32 + 8 * kq) * 8) * 1024 + lane * 16;
  bf16x8 of[64];
  #pragma unroll
  for (int ks = 0; ks < 64; ++ks) of[ks] = *(const bf16x8*)(ob + ks * 1024);
  __syncthreads();
  f32x16 acc[2];
  for (int i = 0; i < 16; ++i) { acc[0][i] = 0.f; acc[1][i] = 0.f; }
  const int b0 = 1024 * hh + 64 * q4 + 8 * (p >> 1);
  const int base0 = b0 + 16 * ((2 * blk + (p & 1)) ^ (2 * hh + 0));
  const int base1 = b0 + 256 + 16 * ((2 * blk + (p & 1)) ^ (2 * hh + 1));
  #pragma unroll
  for (int ks = 0; ks < 64; ++ks) {
    #pragma unroll
    for (int nb = 0; nb < 2; ++nb) {
      bf16x8 a = tr2(lds + base0 + 2048 * ks + 512 * nb, lds + base1 + 2048 * ks + 512 * nb);
      acc[nb] = MFMA(a, of[ks], acc[nb]);
    }
  }
  float* pp = part + ((((size_t)kq * 8 + b) * 4 + w) * 8 + nt) * 2048 + lane * 4;
  #pragma unroll
  for (int nb = 0; nb < 2; ++nb)
    #pragma unroll
    for (int g = 0; g < 4; ++g) {
      float4 v = make_float4(acc[nb][4 * g], acc[nb][4 * g + 1], acc[nb][4 * g + 2], acc[nb][4 * g + 3]);
      *(float4*)(pp + (nb * 4 + g) * 256) = v;
    }
}

__global__ __launch_bounds__(64) void k3_reduce(const float* __restrict__ part, char* __restrict__ out) {
  const int lane = threadIdx.x, r = lane & 31, hh = lane >> 5;
  const int t = blockIdx.x;
  const int nt = t & 7, qb = (t >> 3) & 3, b = t >> 5;
  float acc[32];
  #pragma unroll
  for (int i = 0; i < 32; ++i) acc[i] = 0.f;
  #pragma unroll
  for (int kq = 0; kq < 4; ++kq) {
    const float* pp = part + ((size_t)kq * 256 + t) * 2048 + lane * 4;
    #pragma unroll
    for (int j = 0; j < 8; ++j) { float4 v = *(const float4*)(pp + j * 256); acc[4 * j] += v.x; acc[4 * j + 1] += v.y; acc[4 * j + 2] += v.z; acc[4 * j + 3] += v.w; }
  }
  char* op = out + (size_t)(b * 128 + qb * 32 + r) * 1024 + nt * 128 + hh * 16;
  #pragma unroll
  for (int nb = 0; nb < 2; ++nb)
    #pragma unroll
    for (int s = 0; s < 2; ++s) {
      u32x4 v;
      v[0] = cvtpk(acc[nb * 16 + 8 * s + 0], acc[nb * 16 + 8 * s + 1]); v[1] = cvtpk(acc[nb * 16 + 8 * s + 2], acc[nb * 16 + 8 * s + 3]);
      v[2] = cvtpk(acc[nb * 16 + 8 * s + 4], acc[nb * 16 + 8 * s + 5]); v[3] = cvtpk(acc[nb * 16 + 8 * s + 6], acc[nb * 16 + 8 * s + 7]);
      *(u32x4*)(op + nb * 64 + s * 32) = v;
    }
}

extern "C" void kernel_launch(void* const* d_in, const int* in_sizes, int n_in, void* d_out, int out_size, void* d_ws, size_t ws_size, hipStream_t stream) {
  const char* x = (const char*)d_in[0]; const char* kc = (const char*)d_in[1]; const char* vc = (const char*)d_in[2];
  const char* wq = (const char*)d_in[3]; const char* wo = (const char*)d_in[4];
  char* ofr = (char*)d_ws; float* part = (float*)((char*)d_ws + (8 << 20));
  hipFuncSetAttribute((const void*)k1_attn, hipFuncAttributeMaxDynamicSharedMemorySize, 163840);
  hipFuncSetAttribute((const void*)k2_proj, hipFuncAttributeMaxDynamicSharedMemorySize, 131072);
  k1_attn<<<256, 256, 163840, stream>>>(x, kc, vc, wq, ofr);
  k2_proj<<<256, 256, 131072, stream>>>(ofr, wo, part);
  k3_reduce<<<256, 64, 0, stream>>>(part, (char*)d_out);
}
```

```cpp
#include <hip/hip_runtime.h>
#include <hip/hip_bf16.h>

typedef __attribute__((ext_vector_type(8))) __bf16 bf16x8;
typedef __attribute__((ext_vector_type(2))) __bf16 bf16x2;
typedef __attribute__((ext_vector_type(4))) short s16x4;
typedef __attribute__((ext_vector_type(16))) float f32x16;
typedef __attribute__((ext_vector_type(2))) float f32x2;
typedef __attribute__((ext_vector_type(4))) unsigned u32x4;
typedef unsigned long long u64;
typedef __amdgpu_buffer_rsrc_t rsrc_t;
#define DI __device__ __forceinline__
#define LDSP __attribute__((address_space(3)))
#define GAS __attribute__((address_space(1)))
#define MFMA(a, b, c) __builtin_amdgcn_mfma_f32_32x32x16_bf16((a), (b), (c), 0, 0, 0)
#define GAP() __builtin_amdgcn_sched_barrier(0)
#define BARRIER() asm volatile("s_barrier" ::: "memory")
#define WAITV(n) asm volatile("s_waitcnt vmcnt(" #n ")" ::: "memory")

#define KPOS 1
#define NT_KV 1
#define NT_WQ 0
#define NT_WO 0

constexpr size_t WS_EPOCH = 0;
constexpr size_t WS_FLAG1 = 4096;
constexpr size_t WS_FLAG2 = 4096 + 8192;
constexpr size_t WS_STAMP = 65536;
constexpr size_t WS_OFR = 1 << 20;
constexpr size_t WS_PART = WS_OFR + (8 << 20);

DI unsigned cvtpk(float lo, float hi) { f32x2 v = {lo, hi}; bf16x2 b = __builtin_convertvector(v, bf16x2); return __builtin_bit_cast(unsigned, b); }
DI bf16x8 pack8(const f32x16& a, int s) {
  u32x4 w;
  w[0] = cvtpk(a[8 * s + 0], a[8 * s + 1]); w[1] = cvtpk(a[8 * s + 2], a[8 * s + 3]);
  w[2] = cvtpk(a[8 * s + 4], a[8 * s + 5]); w[3] = cvtpk(a[8 * s + 6], a[8 * s + 7]);
  return __builtin_bit_cast(bf16x8, w);
}
DI bf16x8 tr2(unsigned p0, unsigned p1, int imm) {
  s16x4 lo = __builtin_amdgcn_ds_read_tr16_b64_v4i16((s16x4 LDSP*)(size_t)(p0 + imm));
  s16x4 hi = __builtin_amdgcn_ds_read_tr16_b64_v4i16((s16x4 LDSP*)(size_t)(p1 + imm));
  auto v = __builtin_shufflevector(lo, hi, 0, 1, 2, 3, 4, 5, 6, 7);
  return __builtin_bit_cast(bf16x8, v);
}
DI bf16x8 ld128(unsigned p, int imm) { return *(const bf16x8 LDSP*)(size_t)(p + imm); }
template <int NT> DI void dma(unsigned voff, rsrc_t srd, unsigned soff, unsigned m0v) {
  if (NT) asm volatile("s_mov_b32 m0, %0\n\ts_nop 0\n\tbuffer_load_dwordx4 %1, %2, %3 offen nt lds" :: "s"(m0v), "v"(voff), "s"(srd), "s"(soff) : "memory");
  else    asm volatile("s_mov_b32 m0, %0\n\ts_nop 0\n\tbuffer_load_dwordx4 %1, %2, %3 offen lds" :: "s"(m0v), "v"(voff), "s"(srd), "s"(soff) : "memory");
}
DI u64 rt() { return __builtin_amdgcn_s_memrealtime(); }
#define STAMP(i) do { } while (0)

DI u64 ld_flag(const u64* p) { return __hip_atomic_load((const GAS u64*)p, __ATOMIC_RELAXED, __HIP_MEMORY_SCOPE_AGENT); }
DI void st_flag(u64* p, u64 v) { __hip_atomic_store((GAS u64*)p, v, __ATOMIC_RELAXED, __HIP_MEMORY_SCOPE_AGENT); }

__global__ __launch_bounds__(512, 2) void mha_fused(const char* __restrict__ x, const char* __restrict__ kc, const char* __restrict__ vc,
                                                    const char* __restrict__ wq, const char* __restrict__ wo, char* __restrict__ out, char* __restrict__ ws) {
  extern __shared__ __attribute__((aligned(16))) char smem_[];
  const unsigned lds0 = (unsigned)(size_t)(char LDSP*)smem_;
  const int tid = threadIdx.x, lane = tid & 63, w = __builtin_amdgcn_readfirstlane(tid >> 6);
  const int bid = blockIdx.x, g = bid & 7, idx = bid >> 3, j = idx & 3, b = idx >> 2, h = 4 * g + j;
  const int c0 = (b + 2 * j) & 7;
  const u64 tstart = rt();
  STAMP(0);
  if (w >= 4) {
    const int lw = w - 4;
    u64 tag = 0;
    if (lw == 0) tag = ld_flag((const u64*)(ws + WS_EPOCH + b * 128)) + 1;
    const rsrc_t rx = __builtin_amdgcn_make_buffer_rsrc((void*)x, (short)0, 1 << 20, 0x00020000);
    const rsrc_t rq = __builtin_amdgcn_make_buffer_rsrc((void*)wq, (short)0, 4 << 20, 0x00020000);
    const rsrc_t ro = __builtin_amdgcn_make_buffer_rsrc((void*)wo, (short)0, 4 << 20, 0x00020000);
    const rsrc_t rk = __builtin_amdgcn_make_buffer_rsrc((void*)(kc + (size_t)(b * 32 + h) * 32768), (short)0, 32768, 0x00020000);
    const rsrc_t rv = __builtin_amdgcn_make_buffer_rsrc((void*)(vc + (size_t)(b * 32 + h) * 32768), (short)0, 32768, 0x00020000);
    const int grow = (lane >> 2) & 7, hh = lane >> 5;
    const unsigned cs0 = (4 * hh + ((lane & 3) ^ (grow >> 2))) * 16, cs1 = (4 * hh + ((lane & 3) ^ (2 + (grow >> 2)))) * 16;
    const unsigned vq0 = grow * 8192 + cs0, vq1 = grow * 8192 + cs1;
    const unsigned vx0 = grow * 1024 + cs0, vx1 = grow * 1024 + cs1;
    const unsigned vk0 = grow * 256 + cs0, vk1 = grow * 256 + cs1;
    auto issue_s = [&](int i) {
      const unsigned k0 = ((c0 + i) & 7) * 64, slot = lds0 + (i & 3) * 32768;
      const unsigned sq = (k0 + 16 * lw) * 8192 + h * 256;
      dma<NT_WQ>(vq0, rq, sq, slot + lw * 4096);
      dma<NT_WQ>(vq0, rq, sq + 128, slot + lw * 4096 + 1024);
      dma<NT_WQ>(vq1, rq, sq + 65536, slot + lw * 4096 + 2048);
      dma<NT_WQ>(vq1, rq, sq + 65536 + 128, slot + lw * 4096 + 3072);
      const unsigned sx = (b * 128 + 32 * lw) * 1024 + k0 * 2, dx = slot + 16384 + lw * 4096;
      dma<0>(vx0, rx, sx, dx);
      dma<0>(vx1, rx, sx + 8192, dx + 1024);
      dma<0>(vx0, rx, sx + 16384, dx + 2048);
      dma<0>(vx1, rx, sx + 24576, dx + 3072);
    };
    auto issue_kv = [&](rsrc_t r, unsigned dst) {
      #pragma unroll
      for (int q = 0; q < 4; ++q) {
        const unsigned gg = 4 * lw + q;
        dma<NT_KV>((q & 1) ? vk1 : vk0, r, gg * 2048, dst + gg * 2048);
        dma<NT_KV>((q & 1) ? vk1 : vk0, r, gg * 2048 + 128, dst + gg * 2048 + 1024);
      }
    };
    auto issue_wo = [&](int t, unsigned dst) {
      const unsigned s = (b + t) & 3, row0 = 512 * g + 128 * s;
      #pragma unroll
      for (int q = 0; q < 4; ++q) {
        const unsigned gg = 4 * lw + q;
        const unsigned so = (row0 + 8 * gg) * 1024 + j * 256;
        dma<NT_WO>((q & 1) ? vx1 : vx0, ro, so, dst + gg * 2048);
        dma<NT_WO>((q & 1) ? vx1 : vx0, ro, so + 128, dst + gg * 2048 + 1024);
      }
    };
    const int kpos = KPOS;
    issue_s(0); issue_s(1);
    for (int m = 0; m < 8; ++m) {
      if (kpos == m) WAITV(16); else WAITV(8);
      if (lw == 1) STAMP(m);
      BARRIER();
      if (lw == 1) STAMP(8 + m);
      if (kpos == m + 1) issue_kv(rk, lds0 + 131072);
      if (m < 6) issue_s(m + 2);
      else if (m == 6) issue_kv(rv, lds0);
      else issue_wo(0, lds0 + 32768);
    }
    if (lw != 1) STAMP(2);
    WAITV(8);
    if (lw != 1) STAMP(3);
    BARRIER();
    issue_wo(1, lds0 + 65536); issue_wo(2, lds0 + 98304); issue_wo(3, lds0 + 131072);
    WAITV(0);
    if (lw != 1) STAMP(4);
    BARRIER();
    if (lw != 0) return;
    u64* f1 = (u64*)(ws + WS_FLAG1 + (b * 8 + g) * 128);
    u64* f2 = (u64*)(ws + WS_FLAG2 + (b * 4 + j) * 128);
    BARRIER();
    STAMP(5);
    if (lane == 0) st_flag(f1 + j, tag);
    for (unsigned spins = 0; spins < (1u << 20); ++spins) {
      u64 v = ld_flag(f1 + (lane & 3));
      if (__all(v == tag)) break;
      __builtin_amdgcn_s_sleep(1);
    }
    STAMP(6);
    BARRIER();
    BARRIER();
    STAMP(7);
    if (lane == 0) st_flag(f2 + g, tag);
    for (unsigned spins = 0; spins < (1u << 20); ++spins) {
      u64 v = ld_flag(f2 + (lane & 7));
      if (__all(v == tag)) break;
      __builtin_amdgcn_s_sleep(1);
    }
    STAMP(8);
    BARRIER();
    if (g == 0 && j == 0 && lane == 0) st_flag((u64*)(ws + WS_EPOCH + b * 128), tag);
    return;
  }
  const int r = lane & 31, hh = lane >> 5, blk = (lane >> 4) & 1, q4 = (lane & 15) >> 2, p = lane & 3;
  const unsigned trb = lds0 + 2048 * hh + 64 * q4 + 8 * (p >> 1);
  const unsigned ta0 = trb + 16 * ((2 * blk + (p & 1)) ^ (2 * hh + 0));
  const unsigned ta1 = trb + 256 + 16 * ((2 * blk + (p & 1)) ^ (2 * hh + 1));
  const unsigned ta0h = ta0 + 65536, ta1h = ta1 + 65536;
  const unsigned rowb = 64 * (r & 7);
  const unsigned xb0 = lds0 + 16384 + 4096 * w + 1024 * (r >> 3) + rowb + 16 * ((0 + hh) ^ ((r >> 2) & 3));
  const unsigned xb1 = lds0 + 16384 + 4096 * w + 1024 * (r >> 3) + rowb + 16 * ((2 + hh) ^ ((r >> 2) & 3));
  const unsigned xb0h = xb0 + 65536, xb1h = xb1 + 65536;
  const unsigned kb0 = lds0 + 131072 + 2048 * (r >> 3) + rowb + 16 * ((0 + hh) ^ ((r >> 2) & 3));
  const unsigned kb1 = lds0 + 131072 + 2048 * (r >> 3) + rowb + 16 * ((2 + hh) ^ ((r >> 2) & 3));

  constexpr int DQ = 6;
  constexpr int DS = 6;
  constexpr int DV = 6;
  f32x16 acc[4], s[4];
  bf16x8 A[128], B[32], KF[32], qf[8];
  auto rdA = [&](int t) {
    const int st = t >> 4, ks = (t >> 2) & 3, db = t & 3, imm = (st & 1) * 32768 + 4096 * ks + 512 * db;
    A[t] = (st & 2) ? tr2(ta0h, ta1h, imm) : tr2(ta0, ta1, imm);
  };
  auto rdB = [&](int u) {
    const int st = u >> 2, ks = u & 3, imm = (st & 1) * 32768 + 512 * (ks >> 1);
    B[u] = ld128((st & 2) ? ((ks & 1) ? xb1h : xb0h) : ((ks & 1) ? xb1 : xb0), imm);
  };
  auto rdK = [&](int t) { KF[t] = ld128((t & 1) ? kb1 : kb0, 8192 * (t >> 3) + 512 * ((t & 7) >> 1)); };
  #pragma unroll
  for (int d = 0; d < 4; ++d) for (int i = 0; i < 16; ++i) { acc[d][i] = 0.f; s[d][i] = 0.f; }
  BARRIER();
  STAMP(1);
  rdB(0);
  #pragma unroll
  for (int t = 0; t < DQ; ++t) { if (t == 4) rdB(1); rdA(t); }
  GAP();
  #pragma unroll
  for (int t = 0; t < 128; ++t) {
    acc[t & 3] = MFMA(A[t], B[t >> 2], acc[t & 3]);
    const int tn = t + DQ;
    if (tn < 128) {
      if ((tn & 15) == 0) BARRIER();
      if ((tn & 3) == 0) rdB(tn >> 2);
      rdA(tn);
    } else if (tn - 128 < DS) rdK(tn - 128);
    GAP();
  }
  STAMP(2);
  float m = -3.0e38f;
  qf[0] = pack8(acc[0], 0);
  #pragma unroll
  for (int t = 0; t < 32; ++t) {
    const int kb = t >> 3, ks = t & 7;
    s[kb] = MFMA(KF[t], qf[ks], s[kb]);
    if (t + DS < 32 && t + DS >= DQ) rdK(t + DS);
    if (t < 7) qf[t + 1] = pack8(acc[(t + 1) >> 1], (t + 1) & 1);
    if (kb > 0) { m = fmaxf(fmaxf(m, s[kb - 1][2 * ks]), s[kb - 1][2 * ks + 1]); }
    GAP();
  }
  #pragma unroll
  for (int i = 0; i < 16; ++i) m = fmaxf(m, s[3][i]);
  {
    auto pr = __builtin_amdgcn_permlane32_swap(__float_as_uint(m), __float_as_uint(m), false, false);
    m = fmaxf(__uint_as_float(pr[0]), __uint_as_float(pr[1]));
  }
  const float c = 0.08838834764831845f * 1.4426950408889634f;
  const float mc = m * c;
  bf16x8 pf[8];
  auto expack = [&](int ks) {
    f32x16 t = s[ks >> 1];
    #pragma unroll
    for (int i = 0; i < 8; ++i) t[8 * (ks & 1) + i] = __builtin_amdgcn_exp2f(t[8 * (ks & 1) + i] * c - mc);
    pf[ks] = pack8(t, ks & 1);
  };
  expack(0);
  STAMP(3);
  asm volatile("s_waitcnt lgkmcnt(0)\n\ts_barrier" ::: "memory");
  STAMP(4);
  f32x16 o[4], lacc;
  #pragma unroll
  for (int d = 0; d < 4; ++d) for (int i = 0; i < 16; ++i) o[d][i] = 0.f;
  #pragma unroll
  for (int i = 0; i < 16; ++i) lacc[i] = 0.f;
  {
    const unsigned vb = lds0 + 256 * hh + 64 * q4 + 8 * (p >> 1);
    const unsigned v0 = vb + 16 * ((2 * blk + (p & 1)) ^ (0 + hh));
    const unsigned v1 = vb + 2048 + 16 * ((2 * blk + (p & 1)) ^ (2 + hh));
    u32x4 onesw = {0x3f803f80u, 0x3f803f80u, 0x3f803f80u, 0x3f803f80u};
    const bf16x8 ones = __builtin_bit_cast(bf16x8, onesw);
    bf16x8 VF[32];
    auto rdV = [&](int t) { VF[t] = tr2(v0, v1, 4096 * (t >> 2) + 512 * (t & 3)); };
    #pragma unroll
    for (int t = 0; t < DV; ++t) rdV(t);
    unsigned pw[8][4];
    GAP();
    #pragma unroll
    for (int ks = 0; ks < 8; ++ks) {
      f32x16 e = s[(ks + 1 < 8 ? ks + 1 : 0) >> 1];
      const int so = 8 * ((ks + 1) & 1);
      lacc = MFMA(ones, pf[ks], lacc);
      if (ks < 7) { e[so + 0] = __builtin_amdgcn_exp2f(e[so + 0] * c - mc); e[so + 1] = __builtin_amdgcn_exp2f(e[so + 1] * c - mc); }
      GAP();
      #pragma unroll
      for (int db = 0; db < 4; ++db) {
        const int t = ks * 4 + db;
        o[db] = MFMA(VF[t], pf[ks], o[db]);
        if (t + DV < 32) rdV(t + DV);
        if (ks < 7) {
          if (db < 3) { e[so + 2 + 2 * db] = __builtin_amdgcn_exp2f(e[so + 2 + 2 * db] * c - mc); e[so + 3 + 2 * db] = __builtin_amdgcn_exp2f(e[so + 3 + 2 * db] * c - mc); }
          if (db >= 1) pw[ks + 1][db - 1] = cvtpk(e[so + 2 * (db - 1)], e[so + 2 * (db - 1) + 1]);
          if (db == 3) {
            pw[ks + 1][3] = cvtpk(e[so + 6], e[so + 7]);
            u32x4 wv = {pw[ks + 1][0], pw[ks + 1][1], pw[ks + 1][2], pw[ks + 1][3]};
            pf[ks + 1] = __builtin_bit_cast(bf16x8, wv);
          }
        }
        GAP();
      }
    }
  }
  const float inv = __builtin_amdgcn_rcpf(lacc[0]);
  bf16x8 of[8];
  const rsrc_t rof = __builtin_amdgcn_make_buffer_rsrc((void*)(ws + WS_OFR), (short)0, 8 << 20, 0x00020000);
  #pragma unroll
  for (int ks = 0; ks < 8; ++ks) {
    f32x16 t = o[ks >> 1];
    #pragma unroll
    for (int i = 0; i < 8; ++i) t[8 * (ks & 1) + i] *= inv;
    of[ks] = pack8(t, ks & 1);
    __builtin_amdgcn_raw_buffer_store_b128(__builtin_bit_cast(u32x4, of[ks]), rof, lane * 16, ((b * 32 + h) * 4 + w) * 8192 + ks * 1024, 16);
  }
  STAMP(5);
  BARRIER();
  STAMP(6);
  f32x16 pacc[4];
  #pragma unroll
  for (int d = 0; d < 4; ++d) for (int i = 0; i < 16; ++i) pacc[d][i] = 0.f;
  unsigned wa0[4], wa1[4];
  #pragma unroll
  for (int t = 0; t < 4; ++t) { const unsigned so = ((((j + t) - b) & 3) + 1) * 32768; wa0[t] = ta0 + so; wa1[t] = ta1 + so; }
  bf16x8 W[128], og[4][8];
  auto rdW = [&](int t) { W[t] = tr2(wa0[t >> 5], wa1[t >> 5], 4096 * ((t >> 2) & 7) + 512 * (t & 3)); };
  auto ldO = [&](int t, int ks) {
    og[t][ks] = __builtin_bit_cast(bf16x8, __builtin_amdgcn_raw_buffer_load_b128(rof, lane * 16, ((b * 32 + 4 * g + ((j + t) & 3)) * 4 + w) * 8192 + ks * 1024, 16));
  };
  #pragma unroll
  for (int t = 0; t < DV; ++t) rdW(t);
  GAP();
  #pragma unroll
  for (int t = 0; t < 32; ++t) {
    pacc[t & 3] = MFMA(W[t], of[t >> 2], pacc[t & 3]);
    rdW(t + DV);
    GAP();
  }
  WAITV(0);
  BARRIER();
  BARRIER();
  STAMP(7);
  #pragma unroll
  for (int ks = 0; ks < 8; ++ks) ldO(1, ks);
  #pragma unroll
  for (int ks = 0; ks < 8; ++ks) ldO(2, ks);
  GAP();
  #pragma unroll
  for (int t = 32; t < 128; ++t) {
    pacc[t & 3] = MFMA(W[t], og[t >> 5][(t >> 2) & 7], pacc[t & 3]);
    if (t + DV < 128) rdW(t + DV);
    if (t >= 48 && t < 56) ldO(3, t - 48);
    GAP();
  }
  STAMP(8);
  const rsrc_t rp = __builtin_amdgcn_make_buffer_rsrc((void*)(ws + WS_PART), (short)0, 8 << 20, 0x00020000);
  const unsigned pbase = (b * 4 + j) * 8 * 32768;
  #pragma unroll
  for (int f = 0; f < 8; ++f)
    __builtin_amdgcn_raw_buffer_store_b128(__builtin_bit_cast(u32x4, pack8(pacc[f >> 1], f & 1)), rp, lane * 16, pbase + g * 32768 + w * 8192 + f * 1024, 16);
  WAITV(0);
  BARRIER();
  BARRIER();
  STAMP(9);
  {
    const int qb = g >> 1, f = 4 * (g & 1) + w;
    u32x4 pv[8];
    #pragma unroll
    for (int gs = 0; gs < 8; ++gs) pv[gs] = __builtin_amdgcn_raw_buffer_load_b128(rp, lane * 16, pbase + gs * 32768 + qb * 8192 + f * 1024, 16);
    GAP();
    float a[8];
    #pragma unroll
    for (int e = 0; e < 8; ++e) a[e] = 0.f;
    #pragma unroll
    for (int gs = 0; gs < 8; ++gs)
      #pragma unroll
      for (int e = 0; e < 4; ++e) { a[2 * e] += __uint_as_float(pv[gs][e] << 16); a[2 * e + 1] += __uint_as_float(pv[gs][e] & 0xffff0000u); }
    u32x4 ov;
    #pragma unroll
    for (int e = 0; e < 4; ++e) ov[e] = cvtpk(a[2 * e], a[2 * e + 1]);
    *(u32x4*)(out + (size_t)(b * 128 + 32 * qb + r) * 1024 + j * 256 + f * 32 + hh * 16) = ov;
  }
  STAMP(10);
  while (rt() - tstart < 2200) __builtin_amdgcn_s_sleep(2);
}

extern "C" void kernel_launch(void* const* d_in, const int* in_sizes, int n_in, void* d_out, int out_size, void* d_ws, size_t ws_size, hipStream_t stream) {
  hipFuncSetAttribute((const void*)mha_fused, hipFuncAttributeMaxDynamicSharedMemorySize, 163840);
  mha_fused<<<256, 512, 163840, stream>>>((const char*)d_in[0], (const char*)d_in[1], (const char*)d_in[2], (const char*)d_in[3], (const char*)d_in[4], (char*)d_out, (char*)d_ws);
}
```

```cpp
#include <hip/hip_runtime.h>
#include <hip/hip_bf16.h>

typedef __attribute__((ext_vector_type(8))) __bf16 bf16x8;
typedef __attribute__((ext_vector_type(2))) __bf16 bf16x2;
typedef __attribute__((ext_vector_type(4))) short s16x4;
typedef __attribute__((ext_vector_type(16))) float f32x16;
typedef __attribute__((ext_vector_type(2))) float f32x2;
typedef __attribute__((ext_vector_type(4))) unsigned u32x4;
typedef __attribute__((ext_vector_type(2))) unsigned u32x2;
typedef unsigned long long u64;
typedef __amdgpu_buffer_rsrc_t rsrc_t;
#define DI __device__ __forceinline__
#define LDSP __attribute__((address_space(3)))
#define GAS __attribute__((address_space(1)))
#define MFMA(a, b, c) __builtin_amdgcn_mfma_f32_32x32x16_bf16((a), (b), (c), 0, 0, 0)
#define GAP() __builtin_amdgcn_sched_barrier(0)
#define BARRIER() asm volatile("s_barrier" ::: "memory")
#define WAITV(n) asm volatile("s_waitcnt vmcnt(" #n ")" ::: "memory")
#define WAITL() asm volatile("s_waitcnt lgkmcnt(0)" ::: "memory")

constexpr size_t WS_EPOCH = 0;
constexpr size_t WS_FH = 4096;
constexpr size_t WS_FG = 640 << 10;
constexpr size_t WS_OFR = 1 << 20;
constexpr size_t WS_PART = WS_OFR + (8 << 20);

DI unsigned cvtpk(float lo, float hi) { f32x2 v = {lo, hi}; bf16x2 b = __builtin_convertvector(v, bf16x2); return __builtin_bit_cast(unsigned, b); }
DI bf16x8 pack8(const f32x16& a, int s) {
  u32x4 w;
  w[0] = cvtpk(a[8 * s + 0], a[8 * s + 1]); w[1] = cvtpk(a[8 * s + 2], a[8 * s + 3]);
  w[2] = cvtpk(a[8 * s + 4], a[8 * s + 5]); w[3] = cvtpk(a[8 * s + 6], a[8 * s + 7]);
  return __builtin_bit_cast(bf16x8, w);
}
DI float vmul(float a, float b) { float r; asm("v_mul_f32 %0, %1, %2" : "=v"(r) : "v"(a), "v"(b)); return r; }
DI bf16x8 tr2(unsigned p0, unsigned p1, int imm) {
  s16x4 lo = __builtin_amdgcn_ds_read_tr16_b64_v4i16((s16x4 LDSP*)(size_t)(p0 + imm));
  s16x4 hi = __builtin_amdgcn_ds_read_tr16_b64_v4i16((s16x4 LDSP*)(size_t)(p1 + imm));
  auto v = __builtin_shufflevector(lo, hi, 0, 1, 2, 3, 4, 5, 6, 7);
  return __builtin_bit_cast(bf16x8, v);
}
DI bf16x8 ld128(unsigned p, int imm) { return *(const bf16x8 LDSP*)(size_t)(p + imm); }
template <int NT> DI void dma4(unsigned v0, unsigned v1, unsigned v2, unsigned v3, rsrc_t srd, unsigned soff, unsigned m0v) {
  if (NT) asm volatile("s_mov_b32 m0, %0\n\ts_nop 0\n\t"
                       "buffer_load_dwordx4 %1, %5, %6 offen nt lds\n\tbuffer_load_dwordx4 %2, %5, %6 offen offset:1024 nt lds\n\t"
                       "buffer_load_dwordx4 %3, %5, %6 offen offset:2048 nt lds\n\tbuffer_load_dwordx4 %4, %5, %6 offen offset:3072 nt lds"
                       :: "s"(m0v), "v"(v0), "v"(v1), "v"(v2), "v"(v3), "s"(srd), "s"(soff) : "memory");
  else    asm volatile("s_mov_b32 m0, %0\n\ts_nop 0\n\t"
                       "buffer_load_dwordx4 %1, %5, %6 offen lds\n\tbuffer_load_dwordx4 %2, %5, %6 offen offset:1024 lds\n\t"
                       "buffer_load_dwordx4 %3, %5, %6 offen offset:2048 lds\n\tbuffer_load_dwordx4 %4, %5, %6 offen offset:3072 lds"
                       :: "s"(m0v), "v"(v0), "v"(v1), "v"(v2), "v"(v3), "s"(srd), "s"(soff) : "memory");
}
template <int M> DI void dma8_if(int sel, unsigned v0, unsigned v1, unsigned v2, unsigned v3, rsrc_t srd, unsigned sa, unsigned sb, unsigned ma, unsigned mb) {
  asm volatile("s_cmp_lg_u32 %0, %1\n\ts_cbranch_scc1 1f\n\ts_mov_b32 m0, %2\n\ts_nop 0\n\t"
               "buffer_load_dwordx4 %4, %8, %9 offen nt lds\n\tbuffer_load_dwordx4 %5, %8, %9 offen offset:1024 nt lds\n\t"
               "buffer_load_dwordx4 %6, %8, %9 offen offset:2048 nt lds\n\tbuffer_load_dwordx4 %7, %8, %9 offen offset:3072 nt lds\n\t"
               "s_mov_b32 m0, %3\n\ts_nop 0\n\t"
               "buffer_load_dwordx4 %4, %8, %10 offen nt lds\n\tbuffer_load_dwordx4 %5, %8, %10 offen offset:1024 nt lds\n\t"
               "buffer_load_dwordx4 %6, %8, %10 offen offset:2048 nt lds\n\tbuffer_load_dwordx4 %7, %8, %10 offen offset:3072 nt lds\n1:"
               :: "s"(sel), "n"(M), "s"(ma), "s"(mb), "v"(v0), "v"(v1), "v"(v2), "v"(v3), "s"(srd), "s"(sa), "s"(sb) : "scc", "memory");
}
DI void dma_touch(unsigned vt, rsrc_t srd, unsigned m0v) {
  asm volatile("s_mov_b32 m0, %0\n\ts_nop 0\n\tbuffer_load_dword %1, %2, 0 offen lds" :: "s"(m0v), "v"(vt), "s"(srd) : "memory");
}
template <int M> DI void waitv_sel(unsigned mask) {
  asm volatile("s_waitcnt vmcnt(16)\n\ts_bitcmp1_b32 %0, %1\n\ts_cbranch_scc1 1f\n\ts_waitcnt vmcnt(8)\n1:" :: "s"(mask), "n"(M) : "scc", "memory");
}
DI void ld2s(u64& d, const char* p) { asm volatile("global_load_dwordx2 %0, %1, %2 sc1" : "=v"(d) : "v"(0), "s"(p) : "memory"); }
DI void ld1b(unsigned& d, const char* p) { asm volatile("global_load_dword %0, %1, %2" : "=v"(d) : "v"(0), "s"(p) : "memory"); }
DI unsigned opq(unsigned x) { asm("" : "+v"(x)); return x; }
template <class T> DI void keepv(T& d) { asm volatile("" : "+v"(d)); }
template <int V> struct IC { static constexpr int value = V; };
template <int I, int N, class F> DI void sfor(F&& f) { if constexpr (I < N) { f(IC<I>{}); sfor<I + 1, N>(f); } }
DI u64 ld_flag(const u64* p) { return __hip_atomic_load((const GAS u64*)p, __ATOMIC_RELAXED, __HIP_MEMORY_SCOPE_AGENT); }
DI void st_flag(u64* p, u64 v) { __hip_atomic_store((GAS u64*)p, v, __ATOMIC_RELAXED, __HIP_MEMORY_SCOPE_AGENT); }
DI void st_sc1(u32x4 v, rsrc_t r, unsigned voff) { __builtin_amdgcn_raw_buffer_store_b128(v, r, voff, 0, 16); }
DI u32x4 ld_sc1(rsrc_t r, unsigned voff, unsigned soff) { return __builtin_amdgcn_raw_buffer_load_b128(r, voff, soff, 16); }
DI void publish(rsrc_t r, unsigned voff, u64 tag) { u32x2 t = {(unsigned)tag, (unsigned)(tag >> 32)}; __builtin_amdgcn_raw_buffer_store_b64(t, r, voff, 0, 16); }
DI void await(const u64* p, u64 tag) {
  #pragma unroll 1
  for (unsigned spins = 0; spins < (1u << 18); ++spins) {
    if (__all(ld_flag(p) == tag)) break;
  }
}

constexpr int ord2(int u) {
  if (u < 32) return u;
  if (u < 80) { const int v = u - 32; return (1 + v / 16) * 32 + (v & 15); }
  const int cb = (u - 80) / 12, v = (u - 80) % 12;
  return (1 + v / 4) * 32 + (4 + (v & 3)) * 4 + cb;
}

__global__ __launch_bounds__(512, 2) void mha_fused(const char* __restrict__ x, const char* __restrict__ kc, const char* __restrict__ vc,
                                                    const char* __restrict__ wq, const char* __restrict__ wo, char* __restrict__ out, char* __restrict__ ws) {
  extern __shared__ __attribute__((aligned(16))) char smem_[];
  const unsigned lds0 = (unsigned)(size_t)(char LDSP*)smem_;
  const int tid = threadIdx.x, lane = tid & 63, w = __builtin_amdgcn_readfirstlane(tid >> 6);
  const int bid = blockIdx.x, g = bid & 7, idx = bid >> 3, j = idx & 3, b = idx >> 2, h = 4 * g + j;
  const int c0 = g;
  const int grow = (lane >> 2) & 7, hh = lane >> 5;
  const unsigned cs0 = (4 * hh + ((lane & 3) ^ (grow >> 2))) * 16, cs1 = (4 * hh + ((lane & 3) ^ (2 + (grow >> 2)))) * 16;
  const rsrc_t rp = __builtin_amdgcn_make_buffer_rsrc((void*)(ws + WS_PART), (short)0, 8 << 20, 0x00020000);
  if (w >= 4) {
    const int lw = w - 4;
    const rsrc_t rx = __builtin_amdgcn_make_buffer_rsrc((void*)x, (short)0, 1 << 20, 0x00020000);
    const rsrc_t rq = __builtin_amdgcn_make_buffer_rsrc((void*)(wq - 1024), (short)0, (4 << 20) + 1024, 0x00020000);
    const unsigned vx0 = grow * 1024 + cs0, vx1 = grow * 1024 + cs1 + 7168, vx2 = vx0 + 2 * 7168, vx3 = vx1 + 2 * 7168;
    const unsigned vq0 = 1024 + grow * 8192 + cs0, vq1 = vq0 + 128 - 1024, vq2 = 1024 + grow * 8192 + cs1 + 65536 - 2048, vq3 = vq2 + 128 - 1024;
    const unsigned sxb = (b * 128 + 32 * lw) * 1024, sqb = 16 * lw * 8192 + h * 256, dst = lds0 + lw * 4096;
    auto issue_s = [&](int i) {
      const unsigned k0 = ((c0 + i) & 7) * 64, slot = dst + (i & 3) * 32768;
      dma4<0>(vx0, vx1, vx2, vx3, rx, sxb + k0 * 2, slot + 16384);
      dma4<1>(vq0, vq1, vq2, vq3, rq, sqb + k0 * 8192, slot);
    };
    const rsrc_t ro = __builtin_amdgcn_make_buffer_rsrc((void*)(wo - 1024), (short)0, (4 << 20) + 1024, 0x00020000);
    issue_s(0);
    dma_touch(lane < 32 ? 1024 + 512 * g * 1024 + (idx * 128 + lw * 32 + lane) * 128 : 0x80000000u, ro, lds0 + 131072 + lw * 4096);
    issue_s(1);
    const rsrc_t rk = __builtin_amdgcn_make_buffer_rsrc((void*)(kc + (size_t)(b * 32 + h) * 32768 - 1024), (short)0, 32768 + 1024, 0x00020000);
    const rsrc_t rv = __builtin_amdgcn_make_buffer_rsrc((void*)(vc + (size_t)(b * 32 + h) * 32768 - 1024), (short)0, 32768 + 1024, 0x00020000);
    const unsigned vk = grow * 256 + cs0, vkb = grow * 256 + cs1;
    const unsigned vk0 = 1024 + vk, vk1 = vk0 + 128 - 1024, vk2 = 1024 + vkb, vk3 = vk2 + 128 - 1024;
    const unsigned vo0 = 1024 + grow * 1024 + cs0, vo1 = vo0 + 128 - 1024, vo2 = 1024 + grow * 1024 + cs1 + 8192 - 2048, vo3 = vo2 + 128 - 1024;
    auto issue_wo = [&](int t, unsigned d) {
      const unsigned so = (512 * g + 128 * ((j + t) & 3) + 32 * lw) * 1024 + j * 256;
      dma4<1>(vo0, vo1, vo2, vo3, ro, so, d + lw * 8192);
      dma4<1>(vo0, vo1, vo2, vo3, ro, so + 16384, d + lw * 8192 + 4096);
    };
    u64 ep;
    const int qk = 1 + (b >> 1);
    const unsigned kmask = 6u << qk;
    sfor<0, 8>([&](auto mc) {
      constexpr int m = decltype(mc)::value;
      if constexpr (m == 7) WAITV(9);
      else if constexpr (m == 0) WAITV(9);
      else waitv_sel<m>(kmask);
      BARRIER();
      if constexpr (m < 6) issue_s(m + 2);
      if constexpr (m <= 4) dma8_if<m>(qk, vk0, vk1, vk2, vk3, rk, lw * 4096, lw * 4096 + 16384, lds0 + 131072 + lw * 4096, lds0 + 131072 + lw * 4096 + 16384);
      if constexpr (m == 6) {
        dma4<1>(vk0, vk1, vk2, vk3, rv, lw * 4096, lds0 + lw * 4096);
        dma4<1>(vk0, vk1, vk2, vk3, rv, lw * 4096 + 16384, lds0 + lw * 4096 + 16384);
        ld2s(ep, ws + WS_EPOCH + b * 128);
      }
      if constexpr (m == 7) issue_wo(0, lds0 + 32768);
    });
    WAITV(9);
    BARRIER();
    issue_wo(1, lds0 + 65536); issue_wo(2, lds0 + 98304); issue_wo(3, lds0 + 131072);
    WAITV(24);
    keepv(ep);
    const u64 tag = ep + 1;
    BARRIER();
    BARRIER();
    WAITV(0);
    if (lw == 0) {
      const u64* fh = (const u64*)(ws + WS_FH) + (b * 8 + g) * 16 + (lane & 15) + ((lane >> 4) & 1) * 1024;
      const u64 own = (u64)(0x11111111u << j);
      u64 ok = 0;
      #pragma unroll 1
      for (unsigned spins = 0; spins < (1u << 18); ++spins) {
        ok = __ballot(ld_flag(fh) == tag) | own;
        if ((ok & 0xffff) == 0xffff) break;
      }
      BARRIER();
      #pragma unroll 1
      for (unsigned spins = 0; spins < (1u << 18); ++spins) {
        if ((ok & 0xffff0000u) == 0xffff0000u) break;
        ok = __ballot(ld_flag(fh) == tag) | own;
      }
      BARRIER();
    } else {
      BARRIER();
      BARRIER();
    }
    const int qb = g >> 1, f = 4 * (g & 1) + lw;
    BARRIER();
    if (lw == 0) await((const u64*)(ws + WS_FG + (g & 1) * 131072 + (((b * 4 + j) * 4 + qb) * 8 + (lane & 7)) * 128), tag);
    BARRIER();
    if (h == 0 && lw == 0 && lane == 0) st_flag((u64*)(ws + WS_EPOCH + b * 128), tag);
    const unsigned gbase = (b * 4 + j) * 8 * 32768 + qb * 8192 + f * 1024;
    u32x4 pv[8];
    #pragma unroll
    for (int gs = 0; gs < 8; ++gs) pv[gs] = ld_sc1(rp, lane * 16, gbase + gs * 32768);
    float a[8];
    #pragma unroll
    for (int e = 0; e < 8; ++e) a[e] = 0.f;
    #pragma unroll
    for (int gs = 0; gs < 8; ++gs)
      #pragma unroll
      for (int e = 0; e < 4; ++e) { a[2 * e] += __uint_as_float(pv[gs][e] << 16); a[2 * e + 1] += __uint_as_float(pv[gs][e] & 0xffff0000u); }
    u32x4 ov;
    #pragma unroll
    for (int e = 0; e < 4; ++e) ov[e] = cvtpk(a[2 * e], a[2 * e + 1]);
    *(u32x4*)(out + (size_t)(b * 128 + 32 * qb + (lane & 31)) * 1024 + j * 256 + f * 32 + hh * 16) = ov;
    return;
  }
  const int r = lane & 31, blk = (lane >> 4) & 1, q4 = (lane & 15) >> 2, p = lane & 3;
  const unsigned trb = lds0 + 2048 * hh + 64 * q4 + 8 * (p >> 1);
  const unsigned ta0 = opq(trb + 16 * ((2 * blk + (p & 1)) ^ (2 * hh + 0)));
  const unsigned ta1 = opq(trb + 256 + 16 * ((2 * blk + (p & 1)) ^ (2 * hh + 1)));
  const unsigned ta0h = opq(ta0 + 65536), ta1h = opq(ta1 + 65536);
  const unsigned rowb = 64 * (r & 7);
  const unsigned xb0 = opq(lds0 + 16384 + 4096 * w + 1024 * (r >> 3) + rowb + 16 * ((0 + hh) ^ ((r >> 2) & 3)));
  const unsigned xb1 = opq(lds0 + 16384 + 4096 * w + 1024 * (r >> 3) + rowb + 16 * ((2 + hh) ^ ((r >> 2) & 3)));
  const unsigned xb0h = opq(xb0 + 65536), xb1h = opq(xb1 + 65536);
  const unsigned kb0 = opq(lds0 + 131072 + 2048 * (r >> 3) + rowb + 16 * ((0 + hh) ^ ((r >> 2) & 3)));
  const unsigned kb1 = opq(lds0 + 131072 + 2048 * (r >> 3) + rowb + 16 * ((2 + hh) ^ ((r >> 2) & 3)));

  const rsrc_t rof = __builtin_amdgcn_make_buffer_rsrc((void*)(ws + WS_OFR), (short)0, 8 << 20, 0x00020000);
  const rsrc_t rfh = __builtin_amdgcn_make_buffer_rsrc((void*)(ws + WS_FH), (short)0, 16384, 0x00020000);
  const rsrc_t rfg = __builtin_amdgcn_make_buffer_rsrc((void*)(ws + WS_FG), (short)0, 262144, 0x00020000);
  const unsigned ovo = opq(((b * 32 + h) * 4 + w) * 8192 + lane * 16), l16 = opq(lane * 16);
  const unsigned pvo = opq(((b * 4 + j) * 8 + g) * 32768 + w * 8192 + lane * 16);
  const unsigned fvo = lane == 0 ? 0u : 0x40000000u;
  const unsigned fhv = opq(fvo + (((b * 8 + g) * 4 + w) * 4 + j) * 8), fgv = opq(fvo + (((b * 4 + j) * 4 + w) * 8 + g) * 128);
  unsigned ob[4];
  #pragma unroll
  for (int t = 1; t < 4; ++t) { ob[t] = ((b * 32 + 4 * g + ((j + t) & 3)) * 4 + w) * 8192; asm("" : "+s"(ob[t])); }

  constexpr int DQ = 6;
  constexpr int DS = 6;
  constexpr int DV = 6;
  f32x16 acc[4], s[4];
  bf16x8 A[128], B[32], KF[32], qf[8];
  auto rdA = [&](int t) {
    const int st = t >> 4, ks = (t >> 2) & 3, db = t & 3, imm = (st & 1) * 32768 + 4096 * ks + 512 * db;
    A[t] = (st & 2) ? tr2(ta0h, ta1h, imm) : tr2(ta0, ta1, imm);
  };
  auto rdB = [&](int u) {
    const int st = u >> 2, ks = u & 3, imm = (st & 1) * 32768 + 512 * (ks >> 1);
    B[u] = ld128((st & 2) ? ((ks & 1) ? xb1h : xb0h) : ((ks & 1) ? xb1 : xb0), imm);
  };
  auto rdK = [&](int t) { KF[t] = ld128((t & 1) ? kb1 : kb0, 8192 * (t >> 3) + 512 * ((t & 7) >> 1)); };
  #pragma unroll
  for (int d = 0; d < 4; ++d) for (int i = 0; i < 16; ++i) { acc[d][i] = 0.f; s[d][i] = 0.f; }
  __builtin_amdgcn_s_sleep(10);
  u64 ep;
  unsigned pta, ptb;
  {
    u64 c0a = (u64)(kc + (size_t)(b * 32 + h) * 32768), c1a = (u64)(wo + (size_t)g * 524288), c2a = (u64)(ws + WS_PART + (size_t)((b * 4 + j) * 8 + g) * 32768), c3a = (u64)(ws + WS_FH);
    u64 c0b = (u64)(vc + (size_t)(b * 32 + h) * 32768), c1b = (u64)(ws + WS_OFR + (size_t)(b * 32 + h) * 32768), c2b = (u64)(out + (size_t)b * 131072), c3b = (u64)(ws + WS_FG);
    asm volatile("" : "+s"(c0a), "+s"(c1a), "+s"(c2a), "+s"(c3a), "+s"(c0b), "+s"(c1b), "+s"(c2b), "+s"(c3b));
    u64 pa = (w & 2) ? ((w & 1) ? c3a : c2a) : ((w & 1) ? c1a : c0a), pb = (w & 2) ? ((w & 1) ? c3b : c2b) : ((w & 1) ? c1b : c0b);
    asm volatile("" : "+s"(pa), "+s"(pb));
    ld2s(ep, ws + WS_EPOCH + b * 128);
    ld1b(pta, (const char*)pa); ld1b(ptb, (const char*)pb);
  }
  BARRIER();
  rdB(0);
  #pragma unroll
  for (int t = 0; t < DQ; ++t) { if (t == 4) rdB(1); rdA(t); }
  GAP();
  sfor<0, 128>([&](auto tc) {
    constexpr int t = decltype(tc)::value, tn = t + DQ;
    acc[t & 3] = MFMA(A[t], B[t >> 2], acc[t & 3]);
    if constexpr (tn < 128) {
      if constexpr ((tn & 15) == 0) BARRIER();
      if constexpr ((tn & 3) == 0) rdB(tn >> 2);
      rdA(tn);
    } else {
      if constexpr (tn - 128 < DS) rdK(tn - 128);
    }
    if constexpr (t == 40) { WAITV(0); keepv(ep); keepv(pta); keepv(ptb); }
    GAP();
  });
  const u64 tag = ep + 1;
  constexpr float THR = 8.0f;
  const float c = 0.08838834764831845f * 1.4426950408889634f;
  f32x16 o[4], lacc;
  bf16x8 VF[32], pf[8], of[8];
  const unsigned vb = lds0 + 256 * hh + 64 * q4 + 8 * (p >> 1);
  const unsigned v0 = opq(vb + 16 * ((2 * blk + (p & 1)) ^ (0 + hh)));
  const unsigned v1 = opq(vb + 2048 + 16 * ((2 * blk + (p & 1)) ^ (2 + hh)));
  auto rdV = [&](int ks, int db) { VF[ks * 4 + db] = tr2(v0, v1, 4096 * ks + 512 * db); };
  const unsigned wa0 = opq(ta0 + 32768), wa1 = opq(ta1 + 32768), wa0h = opq(ta0 + 98304), wa1h = opq(ta1 + 98304);
  bf16x8 W[128];
  auto rdW = [&](int t) {
    const int sl = t >> 5, imm = (sl & 1) * 32768 + 4096 * ((t >> 2) & 7) + 512 * (t & 3);
    W[t] = (sl & 2) ? tr2(wa0h, wa1h, imm) : tr2(wa0, wa1, imm);
  };
  #pragma unroll
  for (int d = 0; d < 4; ++d) for (int i = 0; i < 16; ++i) o[d][i] = 0.f;
  #pragma unroll
  for (int i = 0; i < 16; ++i) lacc[i] = 0.f;
  u32x4 onesw = {0x3f803f80u, 0x3f803f80u, 0x3f803f80u, 0x3f803f80u};
  keepv(onesw);
  const bf16x8 ones = __builtin_bit_cast(bf16x8, onesw);
  float ma = -3.0e38f, mb = -3.0e38f, mc1, mc2, inv;
  bool up;
  float e[4][16];
  unsigned pw[8][4];
  auto mx = [&](float& m, int kb, int i) { m = fmaxf(fmaxf(m, s[kb][2 * i]), s[kb][2 * i + 1]); };
  auto rowmax = [&](float m) {
    auto pr = __builtin_amdgcn_permlane32_swap(__float_as_uint(m), __float_as_uint(m), false, false);
    return fmaxf(__uint_as_float(pr[0]), __uint_as_float(pr[1]));
  };
  auto ex2 = [&](int kb, int q) {
    const float mc = kb < 2 ? mc1 : mc2;
    e[kb][2 * q] = __builtin_amdgcn_exp2f(s[kb][2 * q] * c - mc);
    e[kb][2 * q + 1] = __builtin_amdgcn_exp2f(s[kb][2 * q + 1] * c - mc);
  };
  auto pk2 = [&](int kb, int q) {
    const int f = 2 * kb + (q >> 2);
    pw[f][q & 3] = cvtpk(e[kb][2 * q], e[kb][2 * q + 1]);
    if (kb >= 2) keepv(pw[f][q & 3]);
    if ((q & 3) == 3) { u32x4 wv = {pw[f][0], pw[f][1], pw[f][2], pw[f][3]}; pf[f] = __builtin_bit_cast(bf16x8, wv); }
  };
  auto item = [&](auto ic) {
    constexpr int i = decltype(ic)::value;
    if constexpr (i < 16) { ex2(i >> 3, i & 7); if constexpr (i > 0) pk2((i - 1) >> 3, (i - 1) & 7); }
    else if constexpr (i < 20) {
      if constexpr (i == 16) pk2(1, 7);
      sfor<0, 4>([&](auto kc_) { constexpr int k = (i - 16) * 4 + decltype(kc_)::value; mx(mb, 2 + (k >> 3), k & 7); });
    } else if constexpr (i == 20) {
      const float m2 = rowmax(mb) * c;
      up = m2 - mc1 > THR;
      mc2 = up ? m2 : mc1;
    } else if constexpr (i < 37) { ex2(2 + ((i - 21) >> 3), (i - 21) & 7); if constexpr (i > 21) pk2(2 + ((i - 22) >> 3), (i - 22) & 7); }
    else pk2(3, 7);
  };
  unsigned ow[8][4];
  float nm[33][2];
  auto npiece = [&](int q) {
    if (q < 32) {
      const int f = q >> 2, i = q & 3;
      nm[q][0] = vmul(o[f >> 1][8 * (f & 1) + 2 * i], inv); nm[q][1] = vmul(o[f >> 1][8 * (f & 1) + 2 * i + 1], inv);
    }
    if (q > 0) {
      const int f = (q - 1) >> 2, i = (q - 1) & 3;
      ow[f][i] = cvtpk(nm[q - 1][0], nm[q - 1][1]);
      if (i == 3) {
        u32x4 wv = {ow[f][0], ow[f][1], ow[f][2], ow[f][3]};
        of[f] = __builtin_bit_cast(bf16x8, wv);
        st_sc1(wv, rof, ovo + (f >> 2) * 4096 + (f & 3) * 1024);
      }
    }
  };
  auto npieces = [&](auto nc) {
    constexpr int n = decltype(nc)::value;
    sfor<0, 33>([&](auto qc) { constexpr int q = decltype(qc)::value; if constexpr (62 + 4 * (q >> 3) + ((q & 7) + 1) / 2 == n + (q == 32 ? -1 : 0)) npiece(q); });
  };
  qf[0] = pack8(acc[0], 0);
  qf[1] = pack8(acc[0], 1);
  sfor<0, 72>([&](auto nc) {
    constexpr int n = decltype(nc)::value;
    if constexpr (n < 32) s[n >> 3] = MFMA(KF[n], qf[n & 7], s[n >> 3]);
    else {
      constexpr int v = n - 32;
      if constexpr (v == 20) {
        if (__builtin_expect(__any(up), 0)) {
          const float al = __builtin_amdgcn_exp2f(mc1 - mc2);
          #pragma unroll
          for (int d = 0; d < 4; ++d) for (int i = 0; i < 16; ++i) o[d][i] *= al;
          lacc[0] *= al;
        }
        GAP();
      }
      if constexpr (v < 20) {
        constexpr int ks = v / 5, rr = v % 5;
        if constexpr (rr == 0) lacc = MFMA(ones, pf[ks], lacc); else o[rr - 1] = MFMA(VF[ks * 4 + rr - 1], pf[ks], o[rr - 1]);
      } else {
        constexpr int gp = (v - 20) >> 2, ks = 4 + (v & 3);
        if constexpr (gp == 0) lacc = MFMA(ones, pf[ks], lacc); else o[gp - 1] = MFMA(VF[ks * 4 + gp - 1], pf[ks], o[gp - 1]);
      }
    }
    if constexpr (n + DS < 32) rdK(n + DS);
    if constexpr (n == 26) { WAITL(); BARRIER(); }
    sfor<0, 32>([&](auto fc) {
      constexpr int f = decltype(fc)::value, ks = f >> 2, db = f & 3;
      constexpr int use = ks < 4 ? 32 + 5 * ks + 1 + db : 32 + 24 + 4 * db + (ks - 4);
      if constexpr ((use - 6 < 27 ? 27 : use - 6) == n) rdV(ks, db);
    });
    if constexpr (n < 6) qf[n + 2] = pack8(acc[(n + 2) >> 1], n & 1);
    if constexpr (n >= 11 && n < 15) { mx(ma, 0, 2 * (n - 11)); mx(ma, 0, 2 * (n - 11) + 1); }
    if constexpr (n == 19 || n == 20) sfor<0, 4>([&](auto kc_) { mx(ma, 1, (n - 19) * 4 + decltype(kc_)::value); });
    if constexpr (n == 21) mc1 = rowmax(ma) * c;
    if constexpr (n >= 22 && n < 52) sfor<(n - 22) * 38 / 30, (n - 21) * 38 / 30>(item);
    if constexpr (n == 60) { keepv(lacc); inv = __builtin_amdgcn_rcpf(lacc[0]); }
    if constexpr (n == 65) BARRIER();
    if constexpr (n >= 66) rdW(n - 66);
    npieces(nc);
    GAP();
  });
  f32x16 pacc[4];
  #pragma unroll
  for (int d = 0; d < 4; ++d) for (int i = 0; i < 16; ++i) pacc[d][i] = 0.f;
  u32x4 og[4][8];
  auto ldO = [&](int t, int ks) { og[t][ks] = ld_sc1(rof, l16 + (ks & 3) * 1024, ob[t] + (ks >> 2) * 4096); };
  unsigned pw2[8][4];
  auto ppiece = [&](int q) {
    const int f = q >> 2, i = q & 3;
    pw2[f][i] = cvtpk(pacc[f >> 1][8 * (f & 1) + 2 * i], pacc[f >> 1][8 * (f & 1) + 2 * i + 1]);
    if (i == 3) { u32x4 wv = {pw2[f][0], pw2[f][1], pw2[f][2], pw2[f][3]}; st_sc1(wv, rp, pvo + (f >> 2) * 4096 + (f & 3) * 1024); }
  };
  sfor<0, 128>([&](auto uc) {
    constexpr int u = decltype(uc)::value, t = ord2(u), sl = t >> 5, ks = (t >> 2) & 7;
    if constexpr (u == 24) {
      WAITV(4);
      publish(rfh, fhv, tag);
      BARRIER();
    }
    if constexpr (u == 32) {
      WAITV(1);
      publish(rfh, fhv + 8192, tag);
      BARRIER();
      #pragma unroll
      for (int q = 0; q < 4; ++q) ldO(1, q);
      #pragma unroll
      for (int q = 0; q < DV; ++q) rdW(ord2(32 + q));
      GAP();
    }
    if constexpr (sl == 0) pacc[t & 3] = MFMA(W[t], of[ks], pacc[t & 3]);
    else pacc[t & 3] = MFMA(W[t], __builtin_bit_cast(bf16x8, og[sl][ks]), pacc[t & 3]);
    if constexpr (u + DV < 128 && !(u < 32 && u + DV >= 32)) rdW(ord2(u + DV));
    if constexpr (u < 16) npieces(IC<72 + u>{});
    if constexpr (u >= 32 && u < 48 && (u & 1) == 0) ldO(2 + ((u - 32) >> 3), ((u - 32) >> 1) & 3);
    if constexpr (u == 47) BARRIER();
    if constexpr (u >= 48 && u < 72 && (u & 1) == 0) ldO(1 + ((u - 48) >> 3), 4 + (((u - 48) >> 1) & 3));
    sfor<0, 24>([&](auto qc) { constexpr int q = decltype(qc)::value; if constexpr (95 + 12 * (q >> 3) + ((q & 7) + 1) / 2 == u) ppiece(q); });
    GAP();
  });
  #pragma unroll
  for (int q = 24; q < 32; ++q) ppiece(q);
  WAITV(4);
  publish(rfg, fgv, tag);
  WAITV(1);
  publish(rfg, fgv + 131072, tag);
  BARRIER();
  BARRIER();
}

extern "C" void kernel_launch(void* const* d_in, const int* in_sizes, int n_in, void* d_out, int out_size, void* d_ws, size_t ws_size, hipStream_t stream) {
  (void)hipFuncSetAttribute((const void*)mha_fused, hipFuncAttributeMaxDynamicSharedMemorySize, 163840);
  mha_fused<<<256, 512, 163840, stream>>>((const char*)d_in[0], (const char*)d_in[1], (const char*)d_in[2], (const char*)d_in[3], (const char*)d_in[4], (char*)d_out, (char*)d_ws);
}
```
